```python
import torch

def _softmax(x, axis=-1):
    return torch.softmax(x, dim=axis)
def _gelu(x):
    return torch.nn.functional.gelu(x, approximate="tanh")
def _silu(x):
    return torch.nn.functional.silu(x)
def _sigmoid(x):
    return torch.sigmoid(x)
def _layernorm(x, axis=-1, eps=1e-5):
    m = x.mean(dim=axis, keepdim=True)
    v = x.var(dim=axis, keepdim=True, unbiased=False)
    return (x - m) / torch.sqrt(v + eps)
def _scatter_add(tgt, idx, src, axis=0):
    return tgt.clone().index_add_(axis, idx, src)
def _segment_sum(src, seg, n):
    out = torch.zeros((n,) + src.shape[1:], dtype=src.dtype)
    return out.index_add_(0, seg, src)
def _pad(x, pw):
    flat = tuple(v for pair in reversed(pw) for v in pair)
    return torch.nn.functional.pad(x, flat)

kernel_name = 'standalone__low_rank_proj_linformer__prompt'

def setup_inputs():

    _w = (lambda a: (lambda t: t.to(torch.bfloat16) if t.is_floating_point() else t)(torch.as_tensor(a)))
    B, H, Sq, Skv, Dh, R = 1, 32, 2048, 2048, 192, 64
    return {
        "q": _w((lambda *s: torch.randn(*s, dtype=torch.bfloat16) * 0.8)(B, H, Sq, Dh)),
        "k": _w((lambda *s: torch.randn(*s, dtype=torch.bfloat16) * 0.8)(B, H, Skv, Dh)),
        "v": _w((lambda *s: torch.randn(*s, dtype=torch.bfloat16) * 0.05)(B, H, Skv, Dh)),
        "e_proj": _w((lambda *s: torch.randn(*s, dtype=torch.bfloat16) * 0.8)(R, Skv) * (Skv ** -0.5)),
        "f_proj": _w((lambda *s: torch.randn(*s, dtype=torch.bfloat16) * 0.05)(R, Skv) * (Skv ** -0.5)),
    }

def reference(q, k, v, e_proj, f_proj):
    B, H, Sq, Dh = q.shape
    kp = torch.einsum("rn,bhnd->bhrd", e_proj, k)
    vp = torch.einsum("rn,bhnd->bhrd", f_proj, v)
    scores = torch.einsum("bhqd,bhrd->bhqr", q, kp) * (Dh ** -0.5)
    p = _softmax(scores, axis=-1)
    o = torch.einsum("bhqr,bhrd->bhqd", p, vp)
    return o
```

```cpp
#include <hip/hip_runtime.h>
#include <hip/hip_bf16.h>
#include <stdint.h>

typedef short bf16x8 __attribute__((ext_vector_type(8)));
typedef float f32x4 __attribute__((ext_vector_type(4)));
typedef float f32x16 __attribute__((ext_vector_type(16)));
typedef unsigned u32x4 __attribute__((ext_vector_type(4)));
typedef unsigned u32x2 __attribute__((ext_vector_type(2)));

constexpr int H = 32, S = 2048, D = 192, R = 64;

__device__ __forceinline__ unsigned cvtpk(float lo, float hi) {
  typedef __bf16 bf2 __attribute__((ext_vector_type(2)));
  typedef float f2 __attribute__((ext_vector_type(2)));
  f2 v = {lo, hi};
  bf2 r = __builtin_convertvector(v, bf2);
  return __builtin_bit_cast(unsigned, r);
}

__global__ __launch_bounds__(256) void proj_kernel(const uint16_t* __restrict__ k, const uint16_t* __restrict__ v,
                                                   const uint16_t* __restrict__ e, const uint16_t* __restrict__ f,
                                                   uint16_t* __restrict__ ws) {
  __shared__ float red[4][64][68];
  const int bid = blockIdx.x;
  const int cg = bid % 3, tsel = (bid / 3) & 1, head = bid / 6;
  const uint16_t* src = (tsel ? v : k) + (size_t)head * S * D + cg * 64;
  const uint16_t* pe = tsel ? f : e;
  const int tid = threadIdx.x, w = tid >> 6, l = tid & 63, i = l & 15, g = l >> 4;
  f32x4 acc[4][4];
#pragma unroll
  for (int a = 0; a < 4; ++a)
#pragma unroll
    for (int b = 0; b < 4; ++b) acc[a][b] = f32x4{0.f, 0.f, 0.f, 0.f};
#pragma unroll 2
  for (int ks = 0; ks < 16; ++ks) {
    const int n0 = w * 512 + ks * 32;
    u32x2 row[8];
#pragma unroll
    for (int j = 0; j < 8; ++j) row[j] = *(const u32x2*)(src + (size_t)(n0 + 8 * g + j) * D + 4 * i);
    bf16x8 af[4];
#pragma unroll
    for (int mt = 0; mt < 4; ++mt) af[mt] = *(const bf16x8*)(pe + (size_t)(16 * mt + i) * S + n0 + 8 * g);
#pragma unroll
    for (int ee = 0; ee < 4; ++ee) {
      u32x4 fr;
#pragma unroll
      for (int t = 0; t < 4; ++t) {
        unsigned lo = row[2 * t][ee >> 1], hi = row[2 * t + 1][ee >> 1];
        fr[t] = (ee & 1) ? __builtin_amdgcn_perm(hi, lo, 0x07060302u) : __builtin_amdgcn_perm(hi, lo, 0x05040100u);
      }
      bf16x8 bfrag = __builtin_bit_cast(bf16x8, fr);
#pragma unroll
      for (int mt = 0; mt < 4; ++mt) acc[mt][ee] = __builtin_amdgcn_mfma_f32_16x16x32_bf16(af[mt], bfrag, acc[mt][ee], 0, 0, 0);
    }
  }
#pragma unroll
  for (int mt = 0; mt < 4; ++mt)
#pragma unroll
    for (int reg = 0; reg < 4; ++reg) {
      f32x4 x = {acc[mt][0][reg], acc[mt][1][reg], acc[mt][2][reg], acc[mt][3][reg]};
      *(f32x4*)&red[w][16 * mt + 4 * g + reg][4 * i] = x;
    }
  __syncthreads();
  uint16_t* dst = ws + (size_t)head * (2 * R * D) + tsel * (R * D);
  for (int q = tid; q < 512; q += 256) {
    float vals[8];
    int fragidx, lane = q & 63, fl = q >> 6;
    const int rr = lane & 31, hh = lane >> 5;
    if (tsel == 0) {
      const int t = fl >> 2, sl = fl & 3;
      fragidx = t * 12 + cg * 4 + sl;
#pragma unroll
      for (int j = 0; j < 8; ++j) {
        const int r = 32 * t + rr, d = 16 * sl + 8 * hh + j;
        vals[j] = red[0][r][d] + red[1][r][d] + red[2][r][d] + red[3][r][d];
      }
    } else {
      const int dtl = fl >> 2, ks = fl & 3;
      fragidx = (cg * 2 + dtl) * 4 + ks;
#pragma unroll
      for (int j = 0; j < 8; ++j) {
        const int r = 16 * ks + 8 * (j >> 2) + 4 * hh + (j & 3), d = 32 * dtl + rr;
        vals[j] = red[0][r][d] + red[1][r][d] + red[2][r][d] + red[3][r][d];
      }
    }
    u32x4 o = {cvtpk(vals[0], vals[1]), cvtpk(vals[2], vals[3]), cvtpk(vals[4], vals[5]), cvtpk(vals[6], vals[7])};
    *(u32x4*)(dst + (size_t)fragidx * 512 + lane * 8) = o;
  }
}

__global__ __launch_bounds__(256) void attn_kernel(const uint16_t* __restrict__ q, const uint16_t* __restrict__ ws,
                                                   uint16_t* __restrict__ out) {
  __shared__ __attribute__((aligned(16))) uint16_t kv[2 * R * D];
  const int bid = blockIdx.x;
  const int head = bid >> 4, chunk = bid & 15;
  const int tid = threadIdx.x, w = tid >> 6, l = tid & 63, rr = l & 31, hh = l >> 5;
  const int qrow = chunk * 128 + w * 32 + rr;
  const uint16_t* qp = q + ((size_t)head * S + qrow) * D;
  bf16x8 qf[12];
#pragma unroll
  for (int s = 0; s < 12; ++s) qf[s] = *(const bf16x8*)(qp + 16 * s + 8 * hh);
  const uint16_t* src = ws + (size_t)head * (2 * R * D);
#pragma unroll
  for (int it = 0; it < 12; ++it) *(u32x4*)(kv + (it * 256 + tid) * 8) = *(const u32x4*)(src + (it * 256 + tid) * 8);
  __syncthreads();
  f32x16 sacc[2];
#pragma unroll
  for (int t = 0; t < 2; ++t) {
#pragma unroll
    for (int x = 0; x < 16; ++x) sacc[t][x] = 0.f;
#pragma unroll
    for (int s = 0; s < 12; ++s) {
      bf16x8 a = *(const bf16x8*)(kv + (t * 12 + s) * 512 + l * 8);
      sacc[t] = __builtin_amdgcn_mfma_f32_32x32x16_bf16(a, qf[s], sacc[t], 0, 0, 0);
    }
  }
  const float C = 0.07216878364870322f * 1.4426950408889634f;
  float m = sacc[0][0];
#pragma unroll
  for (int x = 1; x < 16; ++x) m = fmaxf(m, sacc[0][x]);
#pragma unroll
  for (int x = 0; x < 16; ++x) m = fmaxf(m, sacc[1][x]);
  m = fmaxf(m, __shfl_xor(m, 32));
  float sum = 0.f;
#pragma unroll
  for (int t = 0; t < 2; ++t)
#pragma unroll
    for (int x = 0; x < 16; ++x) { sacc[t][x] = __builtin_amdgcn_exp2f((sacc[t][x] - m) * C); sum += sacc[t][x]; }
  sum += __shfl_xor(sum, 32);
  const float inv = 1.f / sum;
  bf16x8 pf[4];
#pragma unroll
  for (int ks = 0; ks < 4; ++ks) {
    const int t = ks >> 1, b = (ks & 1) * 8;
    u32x4 p = {cvtpk(sacc[t][b + 0] * inv, sacc[t][b + 1] * inv), cvtpk(sacc[t][b + 2] * inv, sacc[t][b + 3] * inv),
               cvtpk(sacc[t][b + 4] * inv, sacc[t][b + 5] * inv), cvtpk(sacc[t][b + 6] * inv, sacc[t][b + 7] * inv)};
    pf[ks] = __builtin_bit_cast(bf16x8, p);
  }
  uint16_t* op = out + ((size_t)head * S + qrow) * D;
#pragma unroll
  for (int dt = 0; dt < 6; ++dt) {
    f32x16 o;
#pragma unroll
    for (int x = 0; x < 16; ++x) o[x] = 0.f;
#pragma unroll
    for (int ks = 0; ks < 4; ++ks) {
      bf16x8 a = *(const bf16x8*)(kv + R * D + (dt * 4 + ks) * 512 + l * 8);
      o = __builtin_amdgcn_mfma_f32_32x32x16_bf16(a, pf[ks], o, 0, 0, 0);
    }
#pragma unroll
    for (int gq = 0; gq < 4; ++gq) {
      u32x2 st = {cvtpk(o[4 * gq], o[4 * gq + 1]), cvtpk(o[4 * gq + 2], o[4 * gq + 3])};
      *(u32x2*)(op + 32 * dt + 8 * gq + 4 * hh) = st;
    }
  }
}

extern "C" void kernel_launch(void* const* d_in, const int* in_sizes, int n_in, void* d_out, int out_size, void* d_ws,
                              size_t ws_size, hipStream_t stream) {
  const uint16_t* q = (const uint16_t*)d_in[0];
  const uint16_t* k = (const uint16_t*)d_in[1];
  const uint16_t* v = (const uint16_t*)d_in[2];
  const uint16_t* e = (const uint16_t*)d_in[3];
  const uint16_t* f = (const uint16_t*)d_in[4];
  proj_kernel<<<192, 256, 0, stream>>>(k, v, e, f, (uint16_t*)d_ws);
  attn_kernel<<<512, 256, 0, stream>>>(q, (const uint16_t*)d_ws, (uint16_t*)d_out);
}
```

```cpp
#include <hip/hip_runtime.h>
#include <stdint.h>

constexpr int WH = 12;
constexpr int WM = 10;
constexpr int WQM = 6;
constexpr int WQH = 5;

typedef short bf16x8 __attribute__((ext_vector_type(8)));
typedef short s16x4 __attribute__((ext_vector_type(4)));
typedef float f32x2 __attribute__((ext_vector_type(2)));
typedef float f32x4 __attribute__((ext_vector_type(4)));
typedef float f32x16 __attribute__((ext_vector_type(16)));
typedef unsigned u32x4 __attribute__((ext_vector_type(4)));
typedef unsigned u32x8 __attribute__((ext_vector_type(8)));
#define LAS __attribute__((address_space(3)))
#define DI __device__ __forceinline__
#define SB() __builtin_amdgcn_sched_barrier(0)
#define RLX_WG __ATOMIC_RELAXED, __HIP_MEMORY_SCOPE_WORKGROUP

constexpr int NM = 12, NH = 8;
constexpr int SLOT = 32768;
constexpr int KP_OFF = 3 * SLOT, CTL_OFF = KP_OFF + 24576, VP_OFF = CTL_OFF + 1024;
enum { C_MASK = 0, C_ACK = 1, C_RDY = 4  , C_QS = 16  , C_ISS = 20  , C_FULL = 8  , C_FREE = 12   };
constexpr unsigned PAY_BYTES = 128u * 24576u;
constexpr unsigned MB_OFF = PAY_BYTES;
constexpr unsigned NON_OFF = MB_OFF + 256u * 256u;
constexpr unsigned WS_USED = NON_OFF + 32u * 256u;

DI unsigned cvtpk(float lo, float hi) {
  typedef __bf16 bf2 __attribute__((ext_vector_type(2)));
  f32x2 v = {lo, hi};
  return __builtin_bit_cast(unsigned, __builtin_convertvector(v, bf2));
}
template <int N> DI void waitvm() { asm volatile("s_waitcnt vmcnt(%0)" ::"i"(N) : "memory"); }
DI void bar() { asm volatile("s_waitcnt lgkmcnt(0)\n\ts_barrier" ::: "memory"); }
#define DMA_HEAD "s_mov_b32 %0, m0\n\ts_mov_b32 m0, %2\n\t"
#define DMA_TAIL "s_mov_b32 m0, %0"
template <int W, bool NT> DI void dma2(unsigned lds_dst, const char* sb, unsigned v0, unsigned v1) {
  unsigned keep;
  if (NT)
    asm volatile(DMA_HEAD "s_waitcnt vmcnt(%3)\n\tglobal_load_lds_dwordx4 %4, %1 nt\n\t"
                 "s_waitcnt vmcnt(%3)\n\tglobal_load_lds_dwordx4 %5, %1 offset:1024 nt\n\t" DMA_TAIL
                 : "=&s"(keep) : "s"(sb), "s"(lds_dst), "i"(W - 1), "v"(v0), "v"(v1) : "memory");
  else
    asm volatile(DMA_HEAD "s_waitcnt vmcnt(%3)\n\tglobal_load_lds_dwordx4 %4, %1\n\t"
                 "s_waitcnt vmcnt(%3)\n\tglobal_load_lds_dwordx4 %5, %1 offset:1024\n\t" DMA_TAIL
                 : "=&s"(keep) : "s"(sb), "s"(lds_dst), "i"(W - 1), "v"(v0), "v"(v1) : "memory");
}
template <int W> DI void dma4(unsigned lds_dst, const char* sb, unsigned v0, unsigned v1, unsigned v2, unsigned v3) {
  unsigned keep;
  asm volatile(DMA_HEAD "s_waitcnt vmcnt(%3)\n\tglobal_load_lds_dwordx4 %4, %1 nt\n\t"
               "s_waitcnt vmcnt(%3)\n\tglobal_load_lds_dwordx4 %5, %1 offset:1024 nt\n\t"
               "s_waitcnt vmcnt(%3)\n\tglobal_load_lds_dwordx4 %6, %1 offset:2048 nt\n\t"
               "s_waitcnt vmcnt(%3)\n\tglobal_load_lds_dwordx4 %7, %1 offset:3072 nt\n\t" DMA_TAIL
               : "=&s"(keep) : "s"(sb), "s"(lds_dst), "i"(W - 1), "v"(v0), "v"(v1), "v"(v2), "v"(v3) : "memory");
}
DI bf16x8 lds_b128(unsigned a) { return *(const LAS bf16x8*)(size_t)a; }
DI s16x4 lds_tr(unsigned a) { return __builtin_amdgcn_ds_read_tr16_b64_v4i16((LAS s16x4*)(size_t)a); }
DI bf16x8 cat(s16x4 a, s16x4 b) { return __builtin_shufflevector(a, b, 0, 1, 2, 3, 4, 5, 6, 7); }
DI f32x2 unpk(unsigned x) { return f32x2{__uint_as_float(x << 16), __uint_as_float(x & 0xffff0000u)}; }
DI unsigned ctl_get(unsigned ctl, int w) { return (unsigned)__builtin_amdgcn_readfirstlane(__hip_atomic_load((LAS unsigned*)(size_t)(ctl + 4 * w), RLX_WG)); }
DI void ctl_inc(unsigned ctl, int w, int l) {
  asm volatile("" ::: "memory");
  if (l == 0) __hip_atomic_fetch_add((LAS unsigned*)(size_t)(ctl + 4 * w), 1u, RLX_WG);
  asm volatile("" ::: "memory");
}
template <int SLEEP> DI void ctl_wait(unsigned ctl, int w, unsigned want) {
#pragma unroll 1
  for (unsigned spins = 0; spins < (1u << 21); ++spins) {
    if (ctl_get(ctl, w) >= want) break;
    if (SLEEP) __builtin_amdgcn_s_sleep(SLEEP);
  }
  asm volatile("" ::: "memory");
}

DI void ctl_set(unsigned ctl, int w, unsigned val) {
  asm volatile("" ::: "memory");
  __hip_atomic_store((LAS unsigned*)(size_t)(ctl + 4 * w), val, RLX_WG);
  asm volatile("" ::: "memory");
}
template <int SLEEP> DI void ctl_wait4(unsigned ctl, int w, unsigned want) {
#pragma unroll 1
  for (unsigned spins = 0; spins < (1u << 21); ++spins) {
    asm volatile("" ::: "memory");
    const u32x4 x = *(const volatile LAS u32x4*)(size_t)(ctl + 4 * w);
    if ((unsigned)__builtin_amdgcn_readfirstlane(min(min(x[0], x[1]), min(x[2], x[3]))) >= want) break;
    if (SLEEP) __builtin_amdgcn_s_sleep(SLEEP);
  }
  asm volatile("" ::: "memory");
}

template <int V> struct IC { static constexpr int value = V; };

__global__ __launch_bounds__(576) void linformer_fused(const char* __restrict__ q, const char* __restrict__ k,
                                                       const char* __restrict__ v, const char* __restrict__ e,
                                                       const char* __restrict__ f, char* __restrict__ out, char* ws) {
  extern __shared__ __attribute__((aligned(16))) char lds_[];
  u32x8 kargs;
  asm volatile("s_load_dwordx8 %0, %1, 0x8" : "=s"(kargs) : "s"(__builtin_amdgcn_kernarg_segment_ptr()));
  SB();
  const unsigned L = (unsigned)(size_t)lds_;
  const unsigned ctl = L + CTL_OFF;
  const int b = blockIdx.x;
  const int head = (b & 7) + 8 * (b >> 6), j = (b >> 3) & 7;
  const int tsel = j >> 2, sq = j & 3;
  const bool mainw = sq < 3;
  const int ntile = mainw ? NM : NH;
  const int wave = __builtin_amdgcn_readfirstlane(threadIdx.x >> 6);
  const int l = threadIdx.x & 63;
  const auto wsr = __builtin_amdgcn_make_buffer_rsrc(ws, 0, WS_USED, 0x00020000);
  const int cg = wave & 3;
  const bool loader = wave < 4;
  unsigned ko[6];
#pragma unroll
  for (int i = 0; i < 4; ++i) ko[i] = i * 2048 + (l >> 3) * 384 + (((l & 7) ^ (((l >> 4) & 1) << 1) ^ ((i & 1) << 2)) << 4);
  asm volatile("s_waitcnt lgkmcnt(0)" : "+s"(kargs), "+v"(ko[0]), "+v"(ko[1]), "+v"(ko[2]), "+v"(ko[3]));
  if (__builtin_expect(loader, 1)) {
    __builtin_amdgcn_s_setprio(3);
    unsigned eo[2];
    auto calc_eo = [&]() {
#pragma unroll
      for (int i = 0; i < 2; ++i) {
        const int rho = 8 * (2 * cg + i) + (l >> 3), mt = rho >> 4, m = rho & 15;
        const int r = 32 * (mt >> 1) + 8 * (m >> 2) + 4 * (mt & 1) + (m & 3);
        eo[i] = r * 4096 + (((l & 7) ^ (m >> 1)) << 4) - i * 1024;
      }
    };
    auto kptr = [&](int i) { return (const char*)(((unsigned long long)kargs[2 * i + 1] << 32) | kargs[2 * i]); };
    static_assert(WM <= 12 && WH <= 12, "window");
    int k4 = 0;
    if (__builtin_expect(mainw, 1)) {
      const unsigned off0 = head * 786432 + sq * 128 + cg * 12288;
      const char* kb = (tsel ? kptr(1) : kptr(0)) + off0;
      dma4<WM>(L + cg * 4096, kb, ko[0], ko[1], ko[2], ko[3]);
      SB();
      calc_eo();
      const char* eb = tsel ? kptr(3) : kptr(2);
#pragma unroll 1
      for (int t = 0; t < NM; ++t) {
        const unsigned s = L + (k4 < 3 ? k4 : 4) * SLOT;
        k4 = (k4 + 1) & 3;
        if (t >= 4) ctl_wait4<1>(ctl, C_FREE, t - 3);
        if (t) dma4<WM>(s + cg * 4096, kb, ko[0], ko[1], ko[2], ko[3]);
        if (t >= 2) ctl_set(ctl, C_FULL + cg, t - 1);
        dma2<WM, false>(s + 16384 + cg * 2048, eb, eo[0], eo[1]);
        dma2<WM, false>(s + 24576 + cg * 2048, eb + 128, eo[0], eo[1]);
        if (t == 0) bar();
        kb += 49152;
        eb += 256;
      }
    } else {
#pragma unroll
      for (int i = 0; i < 6; ++i) {
        const int idx = 64 * i + l, row = idx / 24, c = idx - 24 * row;
        const int sk = ((row >> 1) & 1) | (((row >> 3) & 1) << 1);
        ko[i] = row * 384 + ((c ^ (sk << 1)) << 4) - (i & 3) * 1024;
      }
      calc_eo();
      const unsigned off0 = head * 786432 + 589824 + cg * 6144;
      const char* kb = (tsel ? kptr(1) : kptr(0)) + off0;
      const char* eb = (tsel ? kptr(3) : kptr(2)) + 3072;
#pragma unroll 1
      for (int t = 0; t < NH; ++t) {
        const unsigned s = L + (k4 < 3 ? k4 : 4) * SLOT;
        k4 = (k4 + 1) & 3;
        if (t >= 4) ctl_wait4<1>(ctl, C_FREE, t - 3);
        dma4<WH>(s + cg * 6144, kb, ko[0], ko[1], ko[2], ko[3]);
        if (t >= 2) ctl_set(ctl, C_FULL + cg, t - 1);
        dma2<WH, true>(s + cg * 6144 + 4096, kb, ko[4], ko[5]);
        dma2<WH, false>(s + 24576 + cg * 2048, eb, eo[0], eo[1]);
        if (t == 0) bar();
        kb += 24576;
        eb += 128;
      }
    }
#pragma unroll
    for (int i = 0; i < 6; ++i) {
      const int idx = 64 * i + l, row = idx / 24, c = idx - 24 * row;
      ko[i] = row * 384 + ((c ^ ((row >> 1) & 7)) << 4) - (i & 3) * 1024;
    }
    const char* qb = q + (size_t)head * 786432 + j * 98304 + cg * 12288;
    const unsigned qs = L + cg * 12288;
    auto qstream = [&](auto WW) {
      constexpr int w = decltype(WW)::value;
      ctl_wait4<1>(ctl, C_FREE, ntile - 2);
      waitvm<8>();
      ctl_set(ctl, C_FULL + cg, ntile - 1);
      dma4<w>(qs, qb, ko[0], ko[1], ko[2], ko[3]);
      dma2<w, true>(qs + 4096, qb, ko[4], ko[5]);
      waitvm<6>();
      ctl_set(ctl, C_FULL + cg, ntile);
      dma2<w, true>(qs + 6144 + 4096, qb + 6144, ko[4], ko[5]);
      ctl_wait<1>(ctl, C_ACK, 4u);
      dma4<w>(qs + 6144, qb + 6144, ko[0], ko[1], ko[2], ko[3]);
      ctl_wait4<1>(ctl, C_FREE, ntile - 1);
      dma4<w>(qs + 49152, qb + 49152, ko[0], ko[1], ko[2], ko[3]);
      dma2<w, true>(qs + 49152 + 4096, qb + 49152, ko[4], ko[5]);
      waitvm<6>();
      ctl_set(ctl, C_QS + cg, 1u);
      if (mainw) ctl_wait<1>(ctl, C_ISS + cg, 1u);
      dma4<w>(qs + 55296, qb + 55296, ko[0], ko[1], ko[2], ko[3]);
      dma2<w, true>(qs + 55296 + 4096, qb + 55296, ko[4], ko[5]);
    };
    if (mainw) qstream(IC<WQM>{}); else qstream(IC<WQH>{});
    __builtin_amdgcn_s_setprio(0);
  } else {
    if (wave == 8 && l < 32) *(LAS unsigned*)(size_t)(ctl + 4 * l) = 0u;
    bar();
  }

  if (wave == 8) {
    __builtin_amdgcn_s_setprio(3);
    const unsigned my_mb = MB_OFF + (unsigned)(head * 8 + j) * 256u;
    const unsigned non_line = NON_OFF + (unsigned)head * 256u;
    const unsigned entry = __builtin_amdgcn_raw_buffer_load_b32(wsr, l < 8 ? my_mb + 4 * l : non_line + 4 * j, 0, 16);
    const unsigned old = __builtin_amdgcn_readlane(entry, 8);
    const unsigned cand = old + 1u + (unsigned)l;
    bool bad = false;
#pragma unroll
    for (int i = 0; i < 8; ++i) bad |= (cand == (unsigned)__builtin_amdgcn_readlane(entry, i));
    const unsigned mine = old + 1u + (unsigned)__builtin_ctzll(__ballot(!bad));
    if (l == 0) __builtin_amdgcn_raw_buffer_store_b32(mine, wsr, non_line + 4 * j, 0, 16);
    {
      const size_t o = (size_t)head * 786432 + j * 98304 + ((l & 1) ? 98300 : 0);
      const char* p = (l & 6) == 0 ? q + o : (l & 6) == 2 ? out + o : (l & 6) == 4 ? ws + (size_t)head * 98304 + ((l & 1) ? 98300 : 0)
                                                                            : (tsel ? v : k) + (size_t)head * 786432 + ((l & 1) ? 786428 : 589820);
      unsigned t = 0;
      if (l < 8) t = __builtin_nontemporal_load((const unsigned*)p);
      asm volatile("" ::"v"(t));
    }
    ctl_wait4<8>(ctl, C_FULL, 5u);
    waitvm<0>();
    const unsigned paddr = (l & 8) ? non_line + 4 * (l & 7) : my_mb + 4 * (l & 7);
    const unsigned faddr = MB_OFF + (unsigned)(head * 8 + (l & 7)) * 256u + j * 4;
    const bool elane = l >= 8 && l < 16;
    unsigned echoed = __builtin_amdgcn_raw_buffer_load_b32(wsr, paddr, 0, 16);
    unsigned ok = 0, shown = 0;
    bool sent = false;
    const char* mbp = ws + my_mb;
    if (mainw) ctl_wait4<8>(ctl, C_FULL, 8u);
#pragma unroll 1
    for (unsigned spins = 0; spins < (1u << 17); ++spins) {
      if (ok != shown) {
        shown = ok;
        __hip_atomic_store((LAS unsigned*)(size_t)(ctl + 4 * C_MASK), ok, RLX_WG);
      }
      if (!sent && (!mainw || ((ok >> (j | 3)) & 1u))) {
        ctl_wait<1>(ctl, C_ACK, 4u);
        if (elane) __builtin_amdgcn_raw_buffer_store_b32(echoed, wsr, faddr, 0, 16);
        sent = true;
        ok |= 1u << j;
      } else {
        if (ok == 0xffu) break;
        __builtin_amdgcn_s_sleep(2);
      }
      if ((spins & 15u) != 15u) {
        u32x8 mb;
        asm volatile("s_load_dwordx8 %0, %1, 0x0 glc\n\ts_waitcnt lgkmcnt(0)" : "=s"(mb) : "s"(mbp) : "memory");
#pragma unroll
        for (int i = 0; i < 8; ++i) ok |= (mb[i] == mine ? 1u : 0u) << i;
      } else {
        asm volatile("" ::: "memory");
        const unsigned x = __builtin_amdgcn_raw_buffer_load_b32(wsr, paddr, 0, 16);
        ok |= (unsigned)__ballot(x == mine) & 0xffu;
        if (sent && elane && x != echoed) __builtin_amdgcn_raw_buffer_store_b32(x, wsr, faddr, 0, 16);
        echoed = x;
      }
    }
    asm volatile("" ::: "memory");
    const unsigned x = __builtin_amdgcn_raw_buffer_load_b32(wsr, paddr, 0, 16);
    if (elane && x != echoed) __builtin_amdgcn_raw_buffer_store_b32(x, wsr, faddr, 0, 16);
    return;
  }
  const int blk = loader ? 4 + cg : cg;
  const u32x4 rs = {(unsigned)(size_t)ws, (unsigned)((size_t)ws >> 32) & 0xffffu, WS_USED, 0x00020000u};

  unsigned ka[2][2], qa[4], wa[4], ob, ob1, ob2, rb, vb;
  auto att_addr = [&]() {
    const int qr = l & 31, h = l >> 5;
    const unsigned qblk = L + blk * 12288;
    const int b16 = (l >> 4) & 1, q4 = (l & 15) >> 2, p4 = l & 3, ps = ((p4 & 1) << 1) | (p4 >> 1);
#pragma unroll
    for (int t = 0; t < 2; ++t)
#pragma unroll
      for (int tt = 0; tt < 2; ++tt) {
        const int fd = (((q4 >> 1) & 1) << 2) | (tt << 1) | (q4 & 1);
        ka[t][tt] = L + KP_OFF + (8 * h + 4 * tt + q4) * 128 + (((4 * t + 2 * b16 + (ps >> 1)) ^ fd) << 4) + 8 * (ps & 1);
      }
#pragma unroll
    for (int i = 0; i < 4; ++i) {
      qa[i] = qblk + qr * 384 + (((2 * i + h) ^ ((qr >> 1) & 7)) << 4);
      wa[i] = qblk + qr * 384 + (((2 * i + h) ^ (qr & 7)) << 4);
    }
    ob = (unsigned)head * 786432u + j * 98304 + blk * 12288 + (l >> 3) * 384 + (l & 7) * 16;
    ob1 = ob + 4096; ob2 = ob + 8192;
    rb = qblk + (l >> 3) * 384 + (((l & 7) ^ (l >> 3)) << 4);
    vb = L + VP_OFF + l * 16;
    asm volatile("" : "+v"(ka[0][0]), "+v"(ka[0][1]), "+v"(ka[1][0]), "+v"(ka[1][1]), "+v"(qa[0]), "+v"(qa[1]), "+v"(qa[2]), "+v"(qa[3]));
    asm volatile("" : "+v"(wa[0]), "+v"(wa[1]), "+v"(wa[2]), "+v"(wa[3]), "+v"(ob), "+v"(ob1), "+v"(ob2), "+v"(rb), "+v"(vb));
  };
  att_addr();
  bf16x8 qf[12];
  auto qload = [&]() {
#pragma unroll
    for (int s = 0; s < 12; ++s) qf[s] = lds_b128(qa[s & 3] + (s >> 2) * 128);
  };

  if (!loader) {
    const int g = l >> 4, qq = (l & 15) >> 2, pp = l & 3, m = l & 15;
    const int sig = ((qq >> 1) & 1) | ((g & 1) << 1);
    unsigned eaddr[2];
#pragma unroll
    for (int ks = 0; ks < 2; ++ks) eaddr[ks] = L + 24576 + m * 128 + (((4 * ks + g) ^ (m >> 1)) << 4);
    const unsigned pbase = (unsigned)(head * 2 + tsel) * 49152u + l * 16;
    int ks4 = 0;
    if (mainw) {
      const unsigned kaddr = L + (8 * g + qq) * 128 + ((cg ^ sig) << 5) + 8 * pp;
      const unsigned pa = pbase + (4 * sq + cg) * 2048;
      f32x4 acc[4];
#pragma unroll
      for (int mt = 0; mt < 4; ++mt) acc[mt] = f32x4{0.f, 0.f, 0.f, 0.f};
      u32x4 hp[2] = {u32x4{0u, 0u, 0u, 0u}, u32x4{0u, 0u, 0u, 0u}};
#define HLOAD(go)                                                                                                       \
  asm volatile("s_cmp_eq_u32 %5, 0\n\ts_cbranch_scc1 .Lhl%=\n\t"                                                        \
               "buffer_load_dwordx4 %0, %2, %3, %4 offen sc1\n\tbuffer_load_dwordx4 %1, %2, %3, %4 offen offset:1024 sc1\n.Lhl%=:" \
               : "+v"(hp[0]), "+v"(hp[1]) : "v"(pa), "s"(rs), "s"(24576), "s"(go) : "memory", "scc")
      unsigned got = 0;
#pragma unroll 1
      for (int t = 0; t < NM; ++t) {
        const unsigned soff = (ks4 < 3 ? ks4 : 4) * SLOT;
        ks4 = (ks4 + 1) & 3;
        ctl_wait4<2>(ctl, C_FULL, t + 1);
        bf16x8 a[4][4], bb[4];
#pragma unroll
        for (int ks = 0; ks < 4; ++ks) {
          bb[ks] = cat(lds_tr(kaddr + soff + (32 * ks) * 128), lds_tr(kaddr + soff + (32 * ks + 4) * 128));
#pragma unroll
          for (int mt = 0; mt < 4; ++mt) a[ks][mt] = lds_b128(eaddr[ks & 1] + soff - 8192 + (ks >> 1) * 8192 + 2048 * mt);
        }
        ctl_set(ctl, C_FREE + cg, t + 1);
        SB();
#pragma unroll
        for (int ks = 0; ks < 4; ++ks)
#pragma unroll
          for (int mt = 0; mt < 4; ++mt) acc[mt] = __builtin_amdgcn_mfma_f32_16x16x32_bf16(a[ks][mt], bb[ks], acc[mt], 0, 0, 0);
        if (t + 1 < NM) {
          const unsigned go = (unsigned)__builtin_amdgcn_readfirstlane(got ? 0u : (ctl_get(ctl, C_MASK) >> (j | 3)) & 1u);
          HLOAD(go);
          got |= go;
        }
      }
      if (!got) {
#pragma unroll 1
        for (unsigned spins = 0; spins < (1u << 21); ++spins) {
          if ((ctl_get(ctl, C_MASK) >> (j | 3)) & 1u) break;
          __builtin_amdgcn_s_sleep(1);
        }
      }
      const unsigned go = (unsigned)__builtin_amdgcn_readfirstlane(got ? 0u : 1u);
      HLOAD(go);
      asm volatile("s_waitcnt vmcnt(0)" : "+v"(hp[0]), "+v"(hp[1])::"memory");
#pragma unroll
      for (int u = 0; u < 2; ++u) {
        u32x4 o;
#pragma unroll
        for (int x = 0; x < 4; ++x) {
          const f32x2 t2 = unpk(hp[u][x]);
          o[x] = cvtpk(acc[2 * u + (x >> 1)][2 * (x & 1)] + t2[0], acc[2 * u + (x >> 1)][2 * (x & 1) + 1] + t2[1]);
        }
        __builtin_amdgcn_raw_buffer_store_b128(o, wsr, pa + u * 1024, 0, 16);
      }
    } else {
      unsigned kaddr[3];
#pragma unroll
      for (int nt = 0; nt < 3; ++nt) kaddr[nt] = L + (8 * g + qq) * 384 + (((3 * cg + nt) ^ sig) << 5) + 8 * pp;
      f32x4 acc[4][3];
#pragma unroll
      for (int mt = 0; mt < 4; ++mt)
#pragma unroll
        for (int nt = 0; nt < 3; ++nt) acc[mt][nt] = f32x4{0.f, 0.f, 0.f, 0.f};
#pragma unroll 1
      for (int t = 0; t < NH; ++t) {
        const unsigned soff = (ks4 < 3 ? ks4 : 4) * SLOT;
        ks4 = (ks4 + 1) & 3;
        ctl_wait4<2>(ctl, C_FULL, t + 1);
        bf16x8 a[2][4], bb[2][3];
#pragma unroll
        for (int ks = 0; ks < 2; ++ks) {
#pragma unroll
          for (int nt = 0; nt < 3; ++nt)
            bb[ks][nt] = cat(lds_tr(kaddr[nt] + soff + (32 * ks) * 384), lds_tr(kaddr[nt] + soff + (32 * ks + 4) * 384));
#pragma unroll
          for (int mt = 0; mt < 4; ++mt) a[ks][mt] = lds_b128(eaddr[ks] + soff + 2048 * mt);
        }
        ctl_set(ctl, C_FREE + cg, t + 1);
        SB();
#pragma unroll
        for (int ks = 0; ks < 2; ++ks)
#pragma unroll
          for (int mt = 0; mt < 4; ++mt)
#pragma unroll
            for (int nt = 0; nt < 3; ++nt)
              acc[mt][nt] = __builtin_amdgcn_mfma_f32_16x16x32_bf16(a[ks][mt], bb[ks][nt], acc[mt][nt], 0, 0, 0);
      }
#pragma unroll
      for (int c = 0; c < 6; ++c) {
        const f32x4 x0 = acc[2 * (c & 1)][c >> 1], x1 = acc[2 * (c & 1) + 1][c >> 1];
        const u32x4 o = {cvtpk(x0[0], x0[1]), cvtpk(x0[2], x0[3]), cvtpk(x1[0], x1[1]), cvtpk(x1[2], x1[3])};
        __builtin_amdgcn_raw_buffer_store_b128(o, wsr, pbase + 24576 + cg * 6144 + c * 1024, 0, 16);
      }
    }
    unsigned gak[6], gav[6];
#pragma unroll
    for (int c = 0; c < 6; ++c) {
      const int u = c & 1, nt = c >> 1;
      const int d = 48 * cg + 16 * nt + m, r8 = 4 * u + g;
      const int fd = (((d >> 1) & 1) << 2) | (((d >> 2) & 1) << 1) | (d & 1);
      gak[c] = L + KP_OFF + d * 128 + ((r8 ^ fd) << 4);
      const int dl = d & 31, rho = (dl & 19) | ((dl & 4) << 1) | ((dl & 8) >> 1);
      gav[c] = L + VP_OFF + ((((d >> 5) * 4 + (r8 >> 1)) * 64 + 32 * (r8 & 1) + rho) << 4);
    }
    unsigned gb = (unsigned)head * 98304u + cg * 6144 + l * 16, gb2 = gb + 4096;
    asm volatile("" : "+v"(gak[0]), "+v"(gak[1]), "+v"(gak[2]), "+v"(gak[3]), "+v"(gak[4]), "+v"(gak[5]), "+v"(gb), "+v"(gb2));
    asm volatile("" : "+v"(gav[0]), "+v"(gav[1]), "+v"(gav[2]), "+v"(gav[3]), "+v"(gav[4]), "+v"(gav[5]));
    waitvm<0>();
    ctl_inc(ctl, C_ACK, l);
    const unsigned need = ((0x4631u >> (4 * cg)) & 7u) * 0x11u;
    ctl_wait4<1>(ctl, C_FREE, ntile);
#pragma unroll 1
    for (unsigned spins = 0; spins < (1u << 21); ++spins) {
      if ((ctl_get(ctl, C_MASK) & need) == need) break;
      __builtin_amdgcn_s_sleep(1);
    }
    asm volatile("" ::: "memory");
    __builtin_amdgcn_s_setprio(3);
    u32x4 pv[12];
#pragma unroll
    for (int c = 0; c < 12; ++c) pv[c] = __builtin_amdgcn_raw_buffer_load_b128(wsr, ((c % 6) < 4 ? gb : gb2) + ((c % 6) & 3) * 1024, (c / 6) * 49152, 16);
    SB();
    ctl_set(ctl, C_ISS + cg, 1u);
    const bool qrdy = ctl_get(ctl, C_QS + cg) != 0u;
    asm volatile("" ::: "memory");
    qload();
    SB();
#pragma unroll
    for (int c = 0; c < 6; ++c) *(LAS u32x4*)(size_t)gak[c] = pv[c];
    ctl_inc(ctl, C_RDY, l);
#pragma unroll
    for (int c = 0; c < 6; ++c) *(LAS u32x4*)(size_t)gav[c] = pv[6 + c];
    ctl_inc(ctl, C_RDY + 1, l);
    __builtin_amdgcn_s_setprio(1);
    if (!qrdy) {
      ctl_wait<1>(ctl, C_QS + cg, 1u);
      qload();
    }
  } else {
    waitvm<0>();
    qload();
  }

  const auto outr = __builtin_amdgcn_make_buffer_rsrc(out, 0, 25165824, 0x00020000);
  {
    bf16x8 af[12][2];
    ctl_wait<0>(ctl, C_RDY, 4u);
    f32x16 sacc[2];
#pragma unroll
    for (int x = 0; x < 16; ++x) { sacc[0][x] = 0.f; sacc[1][x] = 0.f; }
    auto sload = [&](int s) {
      af[s][0] = cat(lds_tr(ka[0][0] + 2048 * s), lds_tr(ka[0][1] + 2048 * s));
      af[s][1] = cat(lds_tr(ka[1][0] + 2048 * s), lds_tr(ka[1][1] + 2048 * s));
      SB();
    };
    sload(0); sload(1); sload(2);
#pragma unroll
    for (int s = 0; s < 12; ++s) {
      sacc[0] = __builtin_amdgcn_mfma_f32_32x32x16_bf16(af[s][0], qf[s], sacc[0], 0, 0, 0);
      sacc[1] = __builtin_amdgcn_mfma_f32_32x32x16_bf16(af[s][1], qf[s], sacc[1], 0, 0, 0);
      SB();
      if (s + 3 < 12) sload(s + 3);
    }
    const bool vrdy = ctl_get(ctl, C_RDY + 1) >= 4u;
    bf16x8 vf[6][4];
    auto vload = [&](int dt) {
#pragma unroll
      for (int ks = 0; ks < 4; ++ks) vf[dt][ks] = lds_b128(vb + (dt * 4 + ks) * 1024);
    };
    asm volatile("" ::: "memory");
    vload(0); vload(1);
    SB();
    const float C = 0.07216878364870322f * 1.4426950408889634f;
    float m0 = fmaxf(sacc[0][0], sacc[1][0]), m1 = fmaxf(sacc[0][1], sacc[1][1]);
#pragma unroll
    for (int x = 2; x < 16; x += 2) {
      m0 = fmaxf(fmaxf(m0, sacc[0][x]), sacc[1][x]);
      m1 = fmaxf(fmaxf(m1, sacc[0][x + 1]), sacc[1][x + 1]);
    }
    float mx = fmaxf(m0, m1);
    {
      auto rr = __builtin_amdgcn_permlane32_swap(__float_as_uint(mx), __float_as_uint(mx), false, false);
      mx = fmaxf(__uint_as_float(rr[0]), __uint_as_float(rr[1]));
    }
    const float mc = -mx * C;
    f32x2 sum2 = {0.f, 0.f};
#pragma unroll
    for (int t = 0; t < 2; ++t)
#pragma unroll
      for (int x = 0; x < 16; x += 2) {
        sacc[t][x] = __builtin_amdgcn_exp2f(fmaf(sacc[t][x], C, mc));
        sacc[t][x + 1] = __builtin_amdgcn_exp2f(fmaf(sacc[t][x + 1], C, mc));
        sum2 += f32x2{sacc[t][x], sacc[t][x + 1]};
      }
    float sum = sum2[0] + sum2[1];
    {
      auto rr = __builtin_amdgcn_permlane32_swap(__float_as_uint(sum), __float_as_uint(sum), false, false);
      sum = __uint_as_float(rr[0]) + __uint_as_float(rr[1]);
    }
    const float inv = __builtin_amdgcn_rcpf(sum);
    bf16x8 pf[4];
#pragma unroll
    for (int ks = 0; ks < 4; ++ks) {
      const int t = ks >> 1, o = (ks & 1) * 8;
      const u32x4 pk = {cvtpk(sacc[t][o + 0] * inv, sacc[t][o + 1] * inv), cvtpk(sacc[t][o + 2] * inv, sacc[t][o + 3] * inv),
                        cvtpk(sacc[t][o + 4] * inv, sacc[t][o + 5] * inv), cvtpk(sacc[t][o + 6] * inv, sacc[t][o + 7] * inv)};
      pf[ks] = __builtin_bit_cast(bf16x8, pk);
    }
    asm volatile("" ::"v"(pf[0]), "v"(pf[1]), "v"(pf[2]), "v"(pf[3]));
    if (!vrdy) {
      ctl_wait<0>(ctl, C_RDY + 1, 4u);
      vload(0); vload(1);
    }
    SB();
#pragma unroll
    for (int cgp = 0; cgp < 3; ++cgp) {
      if (cgp < 2) { vload(2 * cgp + 2); vload(2 * cgp + 3); }
      SB();
      f32x16 o[2];
#pragma unroll
      for (int x = 0; x < 16; ++x) { o[0][x] = 0.f; o[1][x] = 0.f; }
#pragma unroll
      for (int dd = 0; dd < 2; ++dd)
#pragma unroll
        for (int ks = 0; ks < 4; ++ks)
          o[dd] = __builtin_amdgcn_mfma_f32_32x32x16_bf16(vf[2 * cgp + dd][ks], pf[ks], o[dd], 0, 0, 0);
#pragma unroll
      for (int dd = 0; dd < 2; ++dd)
#pragma unroll
        for (int hi = 0; hi < 2; ++hi) {
          const u32x4 w4 = {cvtpk(o[dd][8 * hi + 0], o[dd][8 * hi + 1]), cvtpk(o[dd][8 * hi + 2], o[dd][8 * hi + 3]),
                            cvtpk(o[dd][8 * hi + 4], o[dd][8 * hi + 5]), cvtpk(o[dd][8 * hi + 6], o[dd][8 * hi + 7])};
          *(LAS u32x4*)(size_t)(wa[2 * dd + hi] + cgp * 128) = w4;
        }
      asm volatile("" ::: "memory");
      u32x4 w4[4];
#pragma unroll
      for (int i = 0; i < 4; ++i) w4[i] = *(const LAS u32x4*)(size_t)(rb + i * 3072 + cgp * 128);
      __builtin_amdgcn_raw_buffer_store_b128(w4[0], outr, ob + cgp * 128, 0, 16);
      __builtin_amdgcn_raw_buffer_store_b128(w4[1], outr, ob + 3072 + cgp * 128, 0, 16);
      __builtin_amdgcn_raw_buffer_store_b128(w4[2], outr, ob1 + 2048 + cgp * 128, 0, 16);
      __builtin_amdgcn_raw_buffer_store_b128(w4[3], outr, ob2 + 1024 + cgp * 128, 0, 16);
    }
  }
}

extern "C" void kernel_launch(void* const* d_in, const int* in_sizes, int n_in, void* d_out, int out_size, void* d_ws,
                              size_t ws_size, hipStream_t stream) {
  (void)in_sizes; (void)n_in; (void)out_size; (void)ws_size;
  (void)hipFuncSetAttribute((const void*)linformer_fused, hipFuncAttributeMaxDynamicSharedMemorySize, 163840);
  linformer_fused<<<256, 576, 163840, stream>>>((const char*)d_in[0], (const char*)d_in[1], (const char*)d_in[2],
                                                (const char*)d_in[3], (const char*)d_in[4], (char*)d_out, (char*)d_ws);
}
```
